```python
import torch

def _softmax(x, axis=-1):
    return torch.softmax(x, dim=axis)
def _gelu(x):
    return torch.nn.functional.gelu(x, approximate="tanh")
def _silu(x):
    return torch.nn.functional.silu(x)
def _sigmoid(x):
    return torch.sigmoid(x)
def _layernorm(x, axis=-1, eps=1e-5):
    m = x.mean(dim=axis, keepdim=True)
    v = x.var(dim=axis, keepdim=True, unbiased=False)
    return (x - m) / torch.sqrt(v + eps)
def _scatter_add(tgt, idx, src, axis=0):
    return tgt.clone().index_add_(axis, idx, src)
def _segment_sum(src, seg, n):
    out = torch.zeros((n,) + src.shape[1:], dtype=src.dtype)
    return out.index_add_(0, seg, src)
def _pad(x, pw):
    flat = tuple(v for pair in reversed(pw) for v in pair)
    return torch.nn.functional.pad(x, flat)

kernel_name = 'sequential_preLN__sliding_local_dilated__dense_activation_squared_relu__prompt'

def setup_inputs():

    _w = (lambda a: (lambda t: t.to(torch.bfloat16) if t.is_floating_point() else t)(torch.as_tensor(a)))
    def _A_setup():
        B, S, D = 1, 2048, 640
        return {
            "x": _w((lambda *s: torch.randn(*s, dtype=torch.bfloat16) * 0.05)(B, S, D)),
            "wqkv": _w((lambda *s: torch.randn(*s, dtype=torch.bfloat16) * 0.05)(D, 3 * D)),
            "wo": _w((lambda *s: torch.randn(*s, dtype=torch.bfloat16) * 0.05)(D, D)),
        }
    _A_in = _A_setup()

    def _B_setup():
        B, S, D, F = 1, 2048, 640, 1536
        return {
            "x": _w((lambda *s: torch.randn(*s, dtype=torch.bfloat16) * 0.05)(B, S, D)),
            "w1": _w((lambda *s: torch.randn(*s, dtype=torch.bfloat16) * 0.05)(D, F)),
            "b1": _w((lambda *s: torch.randn(*s, dtype=torch.bfloat16) * 0.4)(F)),
            "w2": _w((lambda *s: torch.randn(*s, dtype=torch.bfloat16) * 0.05)(F, D)),
            "b2": _w((lambda *s: torch.randn(*s, dtype=torch.bfloat16) * 1.6)(D)),
        }
    _B_in = _B_setup()

    return {
        "A_x": _A_in["x"],
        "A_wqkv": _A_in["wqkv"],
        "A_wo": _A_in["wo"],
        "B_x": _B_in["x"],
        "B_w1": _B_in["w1"],
        "B_b1": _B_in["b1"],
        "B_w2": _B_in["w2"],
        "B_b2": _B_in["b2"],
    }

def reference(A_x, A_wqkv, A_wo, B_x, B_w1, B_b1, B_w2, B_b2):
    def _A_ref(x, wqkv, wo):
        B, S, D = x.shape
        H = 32; Dh = D // H
        W, dil = 8, 2
        qkv = (lambda x, *d: x.permute(*d))((x @ wqkv).reshape(B, S, 3, H, Dh), 2, 0, 3, 1, 4)
        q, k, v = qkv[0], qkv[1], qkv[2]
        scale = Dh ** -0.5
        pos = torch.arange(S)
        off = torch.arange(W) * dil
        idx = pos[:, None] - off[None, :]
        mask = idx >= 0
        idx_c = torch.where(mask, idx, 0).reshape(-1)
        kg = k[:, :, idx_c, :].reshape(B, H, S, W, Dh)
        vg = v[:, :, idx_c, :].reshape(B, H, S, W, Dh)
        logits = torch.einsum("bhsd,bhswd->bhsw", q, kg) * scale
        logits = torch.where(mask[None, None, :, :], logits, -1e9)
        attn = _softmax(logits, axis=-1)
        o = torch.einsum("bhsw,bhswd->bhsd", attn, vg)
        return (lambda x, *d: x.permute(*d))(o, 0, 2, 1, 3).reshape(B, S, D) @ wo

    def _B_ref(x, w1, b1, w2, b2):
        h = torch.einsum("bsd,df->bsf", x, w1) + b1
        zero = torch.zeros_like(h)
        r = torch.where(h > zero, h, zero)
        h = r * r
        out = torch.einsum("bsf,fd->bsd", h, w2) + b2
        return out

    x = A_x
    h = x + _A_ref(_layernorm(x, axis=-1), A_wqkv, A_wo)
    h2 = h + _B_ref(_layernorm(h, axis=-1), B_w1, B_b1, B_w2, B_b2)
    return h2
```

```cpp
#include <hip/hip_runtime.h>
#include <hip/hip_bf16.h>
#include <stdint.h>
#include <type_traits>


namespace {

#define LAS __attribute__((address_space(3)))
#define GAS __attribute__((address_space(1)))
typedef short bf16x8 __attribute__((ext_vector_type(8)));
typedef short s16x4 __attribute__((ext_vector_type(4)));
typedef float f32x4 __attribute__((ext_vector_type(4)));
typedef float f32x16 __attribute__((ext_vector_type(16)));
typedef float f32x2 __attribute__((ext_vector_type(2)));
typedef unsigned u32x4 __attribute__((ext_vector_type(4)));
typedef unsigned u32x2 __attribute__((ext_vector_type(2)));
typedef __bf16 bf16x2 __attribute__((ext_vector_type(2)));
typedef __amdgpu_buffer_rsrc_t rsrc_t;
typedef unsigned long long u64;

constexpr int SEQ = 2048, DM = 640, FF = 1536;
constexpr float LN_EPS = 1e-5f;
constexpr int AUX_SC1 = 16;

constexpr int WS_CTRL = 0;
constexpr int WS_O  = 262144;
constexpr int WS_H  = WS_O + 2621440;
constexpr int WS_H1 = WS_H + 2621440;
constexpr int WS_ST = WS_H1 + 6291456;
constexpr int WS_T1 = WS_ST + 327680;
constexpr int WS_END = WS_T1 + 16 * 1152;

constexpr unsigned OOB = 0x7f000000u;
constexpr int LDS_BYTES = 163840;
constexpr int CTL = 162816;
enum { C_PUB0 = 0, C_PUB1, C_PUB2, C_PUB3, C_STG, C_WO, C_RED2  , C_P2MAIN = C_RED2 + 2, C_P2DONE, C_W1, C_CS, C_ST3, C_P3DONE, C_W2PRE, C_PREDONE, C_W2, C_BLK2, C_W2LAST,
       C_P4MAIN, C_RED4  , C_ZERO = 28   };
static_assert(C_RED4 + 2 <= C_ZERO, "control words");
constexpr int CTL_E = CTL + 128;
constexpr int CTL_CS = CTL + 256;

struct Params {
    const void* x; const void* wqkv; const void* wo; const void* w1; const void* b1; const void* w2; const void* b2;
    void* out; void* ws;
};

__device__ __forceinline__ int rfl(int v) { return __builtin_amdgcn_readfirstlane(v); }
__device__ __forceinline__ rsrc_t mk_rsrc(const void* p, int bytes) { return __builtin_amdgcn_make_buffer_rsrc((void*)p, 0, bytes, 0x00020000); }
__device__ __forceinline__ void dma16(unsigned lds, int voff, rsrc_t rs, unsigned soff) {
    asm volatile("s_mov_b32 m0, %0\n\ts_nop 0\n\tbuffer_load_dwordx4 %1, %2, %3 offen lds" :: "s"(lds), "v"(voff), "s"(rs), "s"(soff));
}
template <unsigned LC, unsigned SC>
__device__ __forceinline__ void dma16c(unsigned lb, int voff, rsrc_t rs, unsigned sb) {
    unsigned tmp;
    asm volatile("s_add_u32 m0, %1, %2\n\ts_add_u32 %0, %4, %5\n\tbuffer_load_dwordx4 %3, %6, %0 offen lds"
                 : "=&s"(tmp) : "s"(lb), "i"(LC), "v"(voff), "s"(sb), "i"(SC), "s"(rs) : "scc");
}
template <int... I> struct iseq {};
template <int N, int... I> struct mkseq : mkseq<N - 1, N - 1, I...> {};
template <int... I> struct mkseq<0, I...> { typedef iseq<I...> type; };
template <class F, int... I> __device__ __forceinline__ void static_for_(F f, iseq<I...>) { (f(std::integral_constant<int, I>{}), ...); }
template <int N, class F> __device__ __forceinline__ void static_for(F f) { static_for_(f, typename mkseq<N>::type{}); }
#define WAIT_VM(n) asm volatile("s_waitcnt vmcnt(" #n ")" ::: "memory")
#define WAIT_LGKM0() asm volatile("s_waitcnt lgkmcnt(0)" ::: "memory")
#define BAR_ALL()  asm volatile("s_waitcnt lgkmcnt(0)\n\ts_barrier" ::: "memory")
#define SCHED0() __builtin_amdgcn_sched_barrier(0)

__device__ __forceinline__ f32x4 mfma(bf16x8 a, bf16x8 b, f32x4 c) { return __builtin_amdgcn_mfma_f32_16x16x32_bf16(a, b, c, 0, 0, 0); }
__device__ __forceinline__ f32x16 mfma32(bf16x8 a, bf16x8 b, f32x16 c) { return __builtin_amdgcn_mfma_f32_32x32x16_bf16(a, b, c, 0, 0, 0); }
__device__ __forceinline__ bf16x8 as_frag(u32x4 v) { return __builtin_bit_cast(bf16x8, v); }
__device__ __forceinline__ unsigned pk2(float a, float b) { f32x2 f = {a, b}; return __builtin_bit_cast(unsigned, __builtin_convertvector(f, bf16x2)); }
__device__ __forceinline__ float asf(unsigned u) { return __builtin_bit_cast(float, u); }
__device__ __forceinline__ unsigned asu(float f) { return __builtin_bit_cast(unsigned, f); }
__device__ __forceinline__ float bflo(unsigned u) { return __builtin_bit_cast(float, u << 16); }
__device__ __forceinline__ float bfhi(unsigned u) { return __builtin_bit_cast(float, u & 0xffff0000u); }
__device__ __forceinline__ bf16x8 lds_b128(LAS char* p) { return *(LAS bf16x8*)p; }
__device__ __forceinline__ s16x4 lds_tr(LAS char* p) { return __builtin_amdgcn_ds_read_tr16_b64_v4i16((LAS s16x4*)p); }
__device__ __forceinline__ bf16x8 cat(s16x4 a, s16x4 b) { return __builtin_shufflevector(a, b, 0, 1, 2, 3, 4, 5, 6, 7); }
__device__ __forceinline__ bf16x8 lds_tr2(LAS char* p0, LAS char* p1) { return cat(lds_tr(p0), lds_tr(p1)); }
__device__ __forceinline__ float dot2(unsigned a, unsigned b, float c) {
    return __builtin_amdgcn_fdot2_f32_bf16(__builtin_bit_cast(bf16x2, a), __builtin_bit_cast(bf16x2, b), c, false);
}
__device__ __forceinline__ float red_sum32(float v) {
    auto b = __builtin_amdgcn_permlane32_swap(asu(v), asu(v), false, false);
    return asf(b[0]) + asf(b[1]);
}
__device__ __forceinline__ float red_sum_g(float v) {
    auto a = __builtin_amdgcn_permlane16_swap(asu(v), asu(v), false, false);
    return red_sum32(asf(a[0]) + asf(a[1]));
}
__device__ __forceinline__ float red_max_g(float v) {
    auto a = __builtin_amdgcn_permlane16_swap(asu(v), asu(v), false, false);
    v = fmaxf(asf(a[0]), asf(a[1]));
    auto b = __builtin_amdgcn_permlane32_swap(asu(v), asu(v), false, false);
    return fmaxf(asf(b[0]), asf(b[1]));
}
__device__ __forceinline__ float max3(float a, float b, float c) { float r; asm("v_max3_f32 %0, %1, %2, %3" : "=v"(r) : "v"(a), "v"(b), "v"(c)); return r; }
__device__ __forceinline__ float max2(float a, float b) { float r; asm("v_max_f32 %0, %1, %2" : "=v"(r) : "v"(a), "v"(b)); return r; }
template <bool MAX> __device__ __forceinline__ void red4(float (&v)[4]) {
    auto op = [](float a, float b) { return MAX ? max2(a, b) : a + b; };
    if (MAX) asm volatile("s_nop 1" : "+v"(v[0]), "+v"(v[1]), "+v"(v[2]), "+v"(v[3]));
    auto s0 = __builtin_amdgcn_permlane16_swap(asu(v[0]), asu(v[1]), false, false);
    auto s1 = __builtin_amdgcn_permlane16_swap(asu(v[2]), asu(v[3]), false, false);
    float m = op(asf(s0[0]), asf(s0[1])), n = op(asf(s1[0]), asf(s1[1]));
    if (MAX) asm volatile("s_nop 1" : "+v"(m), "+v"(n));
    auto s2 = __builtin_amdgcn_permlane32_swap(asu(m), asu(n), false, false);
    float r = op(asf(s2[0]), asf(s2[1]));
    if (MAX) asm volatile("s_nop 1" : "+v"(r));
    auto s3 = __builtin_amdgcn_permlane32_swap(asu(r), asu(r), false, false);
    auto s4 = __builtin_amdgcn_permlane16_swap(s3[0], s3[0], false, false);
    auto s5 = __builtin_amdgcn_permlane16_swap(s3[1], s3[1], false, false);
    v[0] = asf(s4[0]); v[1] = asf(s4[1]); v[2] = asf(s5[0]); v[3] = asf(s5[1]);
}
__device__ __forceinline__ void swap16f(float& a, float& b) {
    auto r = __builtin_amdgcn_permlane16_swap(asu(a), asu(b), false, false);
    a = asf(r[0]); b = asf(r[1]);
}
__device__ __forceinline__ void swap32u(unsigned& a, unsigned& b) {
    auto r = __builtin_amdgcn_permlane32_swap(a, b, false, false);
    a = r[0]; b = r[1];
}

__device__ __forceinline__ LAS unsigned* ctl(LAS char* L, int w) { return (LAS unsigned*)(L + CTL + w * 4); }
__device__ __forceinline__ void sig(LAS char* L, int w, int lane) {
    const unsigned addr = (unsigned)(uintptr_t)(L + CTL + w * 4), one = 1u;
    u64 keep;
    asm volatile("s_mov_b64 %0, exec\n\ts_mov_b64 exec, 1\n\tds_add_u32 %1, %2\n\ts_mov_b64 exec, %0" : "=&s"(keep) : "v"(addr), "v"(one) : "memory");
}
template <bool SLEEP>
__device__ __forceinline__ void await(LAS char* L, int w, unsigned n) {
    const unsigned addr = (unsigned)(uintptr_t)(L + CTL + w * 4);
    unsigned v, sv;
    if (SLEEP)
        asm volatile("Lw%=:\n\tds_read_b32 %0, %2\n\ts_waitcnt lgkmcnt(0)\n\tv_readfirstlane_b32 %1, %0\n\ts_cmp_ge_u32 %1, %3\n\ts_cbranch_scc1 Lx%=\n\ts_sleep 1\n\ts_branch Lw%=\nLx%=:"
                     : "=&v"(v), "=&s"(sv) : "v"(addr), "s"(n) : "memory", "scc");
    else
        asm volatile("Lw%=:\n\tds_read_b32 %0, %2\n\ts_waitcnt lgkmcnt(0)\n\tv_readfirstlane_b32 %1, %0\n\ts_cmp_lt_u32 %1, %3\n\ts_cbranch_scc1 Lw%="
                     : "=&v"(v), "=&s"(sv) : "v"(addr), "s"(n) : "memory", "scc");
}
__device__ __forceinline__ unsigned peek(LAS char* L, int w) { return __hip_atomic_load(ctl(L, w), __ATOMIC_RELAXED, __HIP_MEMORY_SCOPE_WORKGROUP); }
__device__ __forceinline__ void await_seen(LAS char* L, int w, unsigned n, unsigned seen) {
    const unsigned addr = (unsigned)(uintptr_t)(L + CTL + w * 4);
    unsigned v = seen, sv;
    asm volatile("s_nop 0\n\tv_readfirstlane_b32 %1, %0\n\ts_cmp_ge_u32 %1, %3\n\ts_cbranch_scc1 Lx%=\nLw%=:\n\tds_read_b32 %0, %2\n\ts_waitcnt lgkmcnt(0)\n\tv_readfirstlane_b32 %1, %0\n\ts_cmp_lt_u32 %1, %3\n\ts_cbranch_scc1 Lw%=\nLx%=:"
                 : "+v"(v), "=&s"(sv) : "v"(addr), "s"(n) : "memory", "scc");
}
__device__ __forceinline__ void publish(LAS char* L, void* ws, u64 e, int g, int m, int phase, int cw, unsigned nwaves, int lane) {
    WAIT_VM(0);
    if (lane == 0) {
        const unsigned old = __hip_atomic_fetch_add(ctl(L, cw), 1u, __ATOMIC_RELAXED, __HIP_MEMORY_SCOPE_WORKGROUP);
        if (old == nwaves - 1) {
            __hip_atomic_store((GAS u64*)((char*)ws + WS_CTRL + g * 32768 + 256 + (phase * 32 + m) * 128), e + 1 + phase, __ATOMIC_RELAXED, __HIP_MEMORY_SCOPE_AGENT);
        }
    }
}
__device__ __forceinline__ void poll(void* ws, u64 e, int g, int phase, unsigned need_mask, int lane) {
    const u64 tag = e + 1 + phase;
    GAS u64* f = (GAS u64*)((char*)ws + WS_CTRL + g * 32768 + 256 + (phase * 32 + (lane & 31)) * 128);
    const bool need = lane < 32 && ((need_mask >> (lane & 31)) & 1u);
    for (unsigned spins = 0; spins < (1u << 16); ++spins) {
        u64 v = tag;
        if (need) v = __hip_atomic_load(f, __ATOMIC_RELAXED, __HIP_MEMORY_SCOPE_AGENT);
        if (__all(v == tag)) break;
        __builtin_amdgcn_s_sleep(1);
    }
    __builtin_amdgcn_fence(__ATOMIC_ACQUIRE, "wavefront");
    asm volatile("" ::: "memory");
}

struct Ids { int g, half, idx; };
__device__ __forceinline__ Ids decode_block() {
    const int b = blockIdx.x, xl = b & 7, l = b >> 3;
    Ids r;
    r.g = (xl >> 1) * 2 + (l >> 4); r.half = xl & 1; r.idx = l & 15;
    return r;
}

constexpr int LAG = 2;
constexpr int P1_SLOT = 34816, P1_WOFF = 18432;
constexpr int P1_RS = 272;
constexpr int P1_STG = 0;
constexpr int P1_STG8 = 152704;
constexpr int P1_OST = 34816;
constexpr int P1_RSX = 139264;
constexpr int P1_FLG = 150784;
constexpr int P1_TAB = 151552;
constexpr int P2_W = 69632;
constexpr int P2_RED = 45056;
constexpr int P3_W = 0;
constexpr int P3_EARLY = 14;
constexpr int P4_RED = 24576;
constexpr int P3_ROWTAB = 159744;
constexpr int P3_BIAS = 160768;
constexpr int TOUCH_LDS2 = 161280;
constexpr int TOUCH_LDS = 157184;
constexpr int HKEEP = 147456;

__device__ __forceinline__ int wimg_voff(int par, int lane, int ct) {
    const int r = lane >> 3, f = ((r >> 1) & 1) | (par << 1);
    return r * 1280 + ct * 128 + (((lane & 7) ^ (f << 1)) * 16);
}
__device__ __forceinline__ int wimg_rd(int lane, int j) {
    const int t = lane & 15, gq = lane >> 4, q = t >> 2, pp = t & 3;
    const int f = ((q >> 1) & 1) | ((gq & 1) << 1);
    return (8 * gq + q) * 128 + (2 * (j ^ f) + (pp >> 1)) * 16 + 8 * (pp & 1);
}
__device__ __forceinline__ void rowtile(int rt, int mh, int& tile0, int& ntile) {
    const int base = rt == 0 ? 0 : (rt == 1 ? 6 : 11);
    tile0 = base + 3 * mh; ntile = (mh == 0 || rt == 0) ? 3 : 2;
}
__device__ __forceinline__ int w1_rd32(int lane) {
    const int G = lane >> 4, q = (lane & 15) >> 2, pp = lane & 3;
    return (8 * (G >> 1) + q) * 192 + (G & 1) * 32 + pp * 8;
}

__device__ __forceinline__ void touch1(unsigned lds, const char* addr) {
    asm volatile("s_mov_b32 m0, %0\n\ts_nop 0\n\tglobal_load_lds_dword %1, off" :: "s"(lds), "v"(addr));
}
template <int CTRL> __device__ __forceinline__ float dpp_add(float v) {
    return v + asf((unsigned)__builtin_amdgcn_update_dpp(0, (int)asu(v), CTRL, 0xf, 0xf, true));
}
__device__ __forceinline__ float row16_sum(float v) {
    v = dpp_add<0x128>(v); v = dpp_add<0x124>(v); v = dpp_add<0x122>(v); return dpp_add<0x121>(v);
}
__device__ __forceinline__ void p1_rowstats_load(const Params& P, LAS char* L, int lane, int a, int T0, int p) {
    const rsrc_t rx = mk_rsrc(P.x, SEQ * DM * 2);
    const int l16 = lane & 15, row = 4 * a + (lane >> 4);
    const int off = (T0 - 16 + 9 * p + row) * 1280 + l16 * 16;
    const unsigned lb = (unsigned)(uintptr_t)L + P1_RSX + a * 5120;
    if (a < 2) static_for<5>([&](auto I) { constexpr int i = I.value; dma16c<1024 * i, 256 * i>(lb, off, rx, 0); });
    else if (lane < 16) static_for<5>([&](auto I) { constexpr int i = I.value; dma16c<256 * i, 256 * i>(lb, off, rx, 0); });
}
__device__ __forceinline__ void p1_rowstats_finish(const Params& P, LAS char* L, int lane, int a, int T0, int p) {
    const rsrc_t rws = mk_rsrc(P.ws, WS_END);
    const int l16 = lane & 15, row = 4 * a + (lane >> 4);
    WAIT_VM(0);
    u32x4 v[5];
#pragma unroll
    for (int i = 0; i < 5; ++i) v[i] = *(LAS u32x4*)(L + P1_RSX + a * 5120 + (a < 2 ? i * 1024 + lane * 16 : i * 256 + l16 * 16));
    float s1 = 0.f, s2 = 0.f;
#pragma unroll
    for (int i = 0; i < 5; ++i)
#pragma unroll
        for (int k = 0; k < 4; ++k) { s1 = dot2(v[i][k], 0x3f803f80u, s1); s2 = dot2(v[i][k], v[i][k], s2); }
    s1 = row16_sum(s1); s2 = row16_sum(s2);
    const float mean = s1 * (1.f / DM);
    const float rstd = __builtin_amdgcn_rsqf(fmaxf(s2 * (1.f / DM) - mean * mean, 0.f) + LN_EPS);
    if (l16 == 0 && row < 9) {
        const u32x2 o = {asu(rstd), asu(-mean * rstd)};
        __builtin_amdgcn_raw_buffer_store_b64(o, rws, WS_T1 + (T0 >> 7) * 1152 + (9 * p + row) * 8, 0, AUX_SC1);
    }
}

__device__ __forceinline__ void p1_compute(const Params& P, LAS char* L, int cw, int lane, int g, int m_id, int T0, int p) {
    if (cw >= 1) p1_rowstats_load(P, L, lane, cw - 1, T0, p);
    else if (lane < 2)
        asm volatile("s_mov_b32 m0, %0\n\ts_nop 0\n\tglobal_load_lds_dword %1, off sc1" :: "s"((unsigned)(uintptr_t)L + CTL_E), "v"((char*)P.ws + WS_CTRL + g * 32768 + lane * 4));
    const int t = lane & 15, gq = lane >> 4;
    int xa[2], wa[2][2];
#pragma unroll
    for (int kh = 0; kh < 2; ++kh) xa[kh] = t * 128 + (((4 * kh + gq) ^ ((t >> 1) & 7)) * 16);
    { const int q = t >> 2, pp = t & 3;
#pragma unroll
      for (int n = 0; n < 2; ++n)
#pragma unroll
        for (int u = 0; u < 2; ++u)
            wa[n][u] = P1_WOFF + (8 * gq + 4 * u + q) * 256 + (((2 * (2 * cw + n) + (pp >> 1)) ^ ((q << 2) | ((2 * gq + u) & 3))) * 16) + 8 * (pp & 1); }
    f32x4 acc[9][2], accs[2];
#pragma unroll
    for (int m = 0; m < 9; ++m) { acc[m][0] = f32x4{0, 0, 0, 0}; acc[m][1] = f32x4{0, 0, 0, 0}; }
    f32x2 tb[9];
#pragma unroll
    for (int m = 0; m < 9; ++m) tb[m] = f32x2{0.f, 0.f};
#pragma unroll 1
    for (int s = 0; s < 10; ++s) {
        BAR_ALL();
        LAS char* sl = L + (s & 3) * P1_SLOT;
        s16x4 w0[4], w1[4]; bf16x8 x0[9], x1[9];
#pragma unroll
        for (int i = 0; i < 4; ++i) w0[i] = lds_tr(sl + wa[i >> 1][i & 1]);
        x0[0] = lds_b128(sl + xa[0]);
        x0[1] = lds_b128(sl + xa[0] + 2048);
        x0[2] = lds_b128(sl + xa[0] + 2 * 2048);
        SCHED0();
        const bf16x8 wf0[2] = {cat(w0[0], w0[1]), cat(w0[2], w0[3])};
#pragma unroll
        for (int m = 0; m < 9; ++m) {
            acc[m][0] = mfma(wf0[0], x0[m], acc[m][0]);
            SCHED0();
            { const int i = 2 * m;     if (i < 6) x0[3 + i] = lds_b128(sl + xa[0] + (3 + i) * 2048); else if (i < 10) w1[i - 6] = lds_tr(sl + wa[(i - 6) >> 1][(i - 6) & 1] + 8192); else x1[i - 10] = lds_b128(sl + xa[1] + (i - 10) * 2048); }
            SCHED0();
            acc[m][1] = mfma(wf0[1], x0[m], acc[m][1]);
            SCHED0();
            { const int i = 2 * m + 1; if (i < 6) x0[3 + i] = lds_b128(sl + xa[0] + (3 + i) * 2048); else if (i < 10) w1[i - 6] = lds_tr(sl + wa[(i - 6) >> 1][(i - 6) & 1] + 8192); else x1[i - 10] = lds_b128(sl + xa[1] + (i - 10) * 2048); }
            SCHED0();
        }
        x1[8] = lds_b128(sl + xa[1] + 8 * 2048);
        if (s == 9) {
#pragma unroll
            for (int m = 0; m < 9; ++m) tb[m] = *(LAS f32x2*)(L + P1_TAB + (16 * m + t) * 8);
        }
        SCHED0();
        const bf16x8 wf1[2] = {cat(w1[0], w1[1]), cat(w1[2], w1[3])};
#pragma unroll
        for (int m = 0; m < 9; ++m) {
            acc[m][0] = mfma(wf1[0], x1[m], acc[m][0]);
            acc[m][1] = mfma(wf1[1], x1[m], acc[m][1]);
        }
        if (s == 0) { if (cw >= 1) p1_rowstats_finish(P, L, lane, cw - 1, T0, p); else WAIT_VM(0); }
        if (s == 1 && cw >= 1) publish(L, P.ws, *(LAS u64*)(L + CTL_E), g, m_id, 3, C_PUB0, 3, lane);
    }
#pragma unroll
    for (int n = 0; n < 2; ++n)
#pragma unroll
        for (int r = 0; r < 4; ++r) accs[n][r] = asf((unsigned)__builtin_amdgcn_update_dpp(0, (int)asu(acc[0][n][r]), 0x150, 0xf, 0xf, false));
    asm volatile("s_nop 7\n\ts_nop 7" ::: "memory");
    SCHED0();
    {
        LAS char* dst[2];
#pragma unroll
        for (int n = 0; n < 2; ++n) {
            const int u = 4 * (2 * cw + n) + gq;
            const int sec = u / 5, pos = u - sec * 5;
            dst[n] = L + t * P1_RS + (u < 20 ? sec * 48 + pos * 8 : (u < 30 ? 192 + (u - 20) * 8 : (u - 30) * 48 + 40));
        }
        f32x2 tl[18], th[18], lo[18], hi[18]; u32x2 w[18];
        static_for<20>([&](auto I) {
            constexpr int i = I.value;
            if constexpr (i < 18) {
                constexpr int n = i / 9, m = i % 9;
                const f32x2 cl = {accs[n][0], accs[n][1]}, ch = {accs[n][2], accs[n][3]};
                asm("v_pk_mul_f32 %0, %1, %2 op_sel:[0,1] op_sel_hi:[1,1]" : "=v"(tl[i]) : "v"(cl), "v"(tb[m]));
                asm("v_pk_mul_f32 %0, %1, %2 op_sel:[0,1] op_sel_hi:[1,1]" : "=v"(th[i]) : "v"(ch), "v"(tb[m]));
            }
            if constexpr (i >= 1 && i < 19) {
                constexpr int j = i - 1, n = j / 9, m = j % 9;
                const f32x2 a0 = {acc[m][n][0], acc[m][n][1]}, a1 = {acc[m][n][2], acc[m][n][3]};
                asm("v_pk_fma_f32 %0, %1, %2, %3 op_sel_hi:[1,0,1]" : "=v"(lo[j]) : "v"(a0), "v"(tb[m]), "v"(tl[j]));
                asm("v_pk_fma_f32 %0, %1, %2, %3 op_sel_hi:[1,0,1]" : "=v"(hi[j]) : "v"(a1), "v"(tb[m]), "v"(th[j]));
            }
            if constexpr (i >= 2) {
                constexpr int j = i - 2, n = j / 9, m = j % 9;
                w[j].x = pk2(lo[j].x, lo[j].y); w[j].y = pk2(hi[j].x, hi[j].y);
                *(LAS u32x2*)(dst[n] + (m < 8 ? P1_STG + m * 16 * P1_RS : P1_STG8)) = w[j];
            }
            if constexpr (i >= 5) asm volatile("" :: "v"(w[i - 5]));
            SCHED0();
        });
    }
    asm volatile("" ::: "memory");
    sig(L, C_STG, lane);
}

__device__ __forceinline__ void p1_helper(const Params& P, LAS char* L, int hw, int lane, int g, int m_id, int tt, int T0, int p, bool has_p2, int ct) {
    const unsigned lbase = (unsigned)(uintptr_t)L;
    const rsrc_t rx = mk_rsrc(P.x, SEQ * DM * 2), rw = mk_rsrc(P.wqkv, DM * 3 * DM * 2), rwo = mk_rsrc(P.wo, DM * DM * 2), rws = mk_rsrc(P.ws, WS_END);
    const int xrow = 8 * hw + (lane >> 3);
    const int xv0 = (T0 - 16 + xrow) * 1280 + (((lane & 7) ^ ((xrow >> 1) & 7)) * 16);
    const int xv1 = xv0 + 40960;
    int wv;
    { const int wr = lane >> 4, wc = (lane & 15) ^ ((wr << 2) | hw);
      const int seg = wc / 5, cc = wc - seg * 5;
      wv = wc == 15 ? (int)OOB : (4 * hw + wr) * 3840 + seg * 1280 + p * 80 + cc * 16; }
    auto issue = [&](int tk) {
        const unsigned sl = lbase + (tk & 3) * P1_SLOT + hw * 1024;
        const unsigned xs = tk * 128, ws0 = 64 * tk * 3840;
        if (hw == 0) {
            unsigned keep;
            asm volatile("s_mov_b32 %0, exec_lo\n\ts_mov_b32 m0, %1\n\ts_mov_b32 exec_lo, 0xffffff00\n\tbuffer_load_dwordx4 %2, %4, %3 offen lds\n\ts_mov_b32 exec_lo, %0"
                         : "=&s"(keep) : "s"(sl), "v"(xv0), "s"(xs), "s"(rx));
        } else dma16c<0, 0>(sl, xv0, rx, xs);
        static_for<4>([&](auto J) {
            constexpr int j = J.value;
            dma16c<P1_WOFF + 4096 * j, j * 16 * 3840>(sl, wv, rw, ws0);
            if (j < 3 || hw < 2) dma16c<4096 * (j + 1), j * 40960>(sl, xv1, rx, xs);
        });
    };
    const unsigned scratch = lbase + TOUCH_LDS;
    const bool fast = (0xa5 >> (p & 7)) & 1;
    const int fx = ((m_id >> 3) + 4 * g) * 4 + ((p & 7) == 0 ? 0 : ((p & 7) == 2 ? 1 : ((p & 7) == 5 ? 2 : 3)));
#pragma unroll 1
    for (int tk = 0; tk < LAG; ++tk) {
        issue(tk);
        if (tk == 0 && fast) {
            if (hw == 0) { touch1(scratch, (const char*)P.wqkv + (size_t)(fx * 150 + lane) * 128); touch1(scratch, (const char*)P.wqkv + (size_t)(fx * 150 + 64 + lane) * 128); }
            else if (hw == 1) { touch1(scratch, (const char*)P.x + (size_t)(fx * 160 + lane) * 128); touch1(scratch, (const char*)P.x + (size_t)(fx * 160 + 64 + lane) * 128); }
            else if (hw == 2) touch1(scratch, lane < 32 ? (const char*)P.x + (size_t)(fx * 160 + 128 + lane) * 128 : (const char*)P.wqkv + (size_t)(fx * 150 + 128 + (lane < 54 ? lane - 32 : 21)) * 128);
        }
    }
#pragma unroll 1
    for (int s = 0; s < 10; ++s) {
        const int younger = (s + LAG - 1 > 9 ? 9 : s + LAG - 1) - s;
        if (younger == 2) { if (hw < 2) WAIT_VM(18); else WAIT_VM(16); }
        else if (younger == 1) { if (hw < 2) WAIT_VM(9); else WAIT_VM(8); }
        else WAIT_VM(0);
        BAR_ALL();
        if (s + LAG < 10) issue(s + LAG);
        if (s == 1 && fast) {
            if (hw == 0) { touch1(scratch, (const char*)P.w1 + (size_t)(fx * 120 + lane) * 128); touch1(scratch, (const char*)P.w1 + (size_t)(fx * 120 + 64 + (lane < 56 ? lane : 55)) * 128); }
            else if (hw == 1) { touch1(scratch, (const char*)P.w2 + (size_t)(fx * 120 + lane) * 128); touch1(scratch, (const char*)P.w2 + (size_t)(fx * 120 + 64 + (lane < 56 ? lane : 55)) * 128); }
            else if (hw == 2) touch1(scratch, (const char*)P.wo + (size_t)(fx * 50 + (lane < 50 ? lane : 49)) * 128);
            else touch1(scratch, lane < 24 ? (const char*)P.b1 + lane * 128 : (const char*)P.b2 + (lane < 34 ? lane - 24 : 9) * 128);
        }
        if (hw == 0) {
            GAS u64* f = (GAS u64*)((char*)P.ws + WS_CTRL + g * 32768 + 256 + (3 * 32 + ((lane & 8) << 1) + 8 * tt + (lane & 7)) * 128);
            if (s == 3 && lane < 16) asm volatile("s_mov_b32 m0, %0\n\ts_nop 0\n\tglobal_load_lds_dwordx4 %1, off sc1" :: "s"(lbase + P1_FLG), "v"(f) : "memory");
            if (s == 5) {
                const u64 fv = *(LAS u64*)(L + P1_FLG + (lane & 15) * 16);
                const u64 tag = *(LAS u64*)(L + CTL_E) + 4;
                if (__builtin_expect(!__all(fv == tag), 0)) {
                    for (unsigned spins = 0; spins < (1u << 16); ++spins)
                        if (__all(__hip_atomic_load(f, __ATOMIC_RELAXED, __HIP_MEMORY_SCOPE_AGENT) == tag)) break;
                }
                asm volatile("" ::: "memory");
                const unsigned tb = lbase + P1_TAB, so = WS_T1 + (T0 >> 7) * 1152;
                asm volatile("s_mov_b32 m0, %0\n\ts_nop 0\n\tbuffer_load_dwordx4 %1, %2, %3 offen sc1 lds" :: "s"(tb), "v"(lane * 16), "s"(rws), "s"(so) : "memory");
                if (lane < 8) asm volatile("s_mov_b32 m0, %0\n\ts_nop 0\n\tbuffer_load_dwordx4 %1, %2, %3 offen sc1 lds" :: "s"(tb + 1024), "v"(lane * 16), "s"(rws), "s"(so + 1024) : "memory");
            }
        }
    }
    if (has_p2) {
        const int wov = wimg_voff(hw & 1, lane, ct);
        static_for<20>([&](auto I) { constexpr int i = I.value; dma16c<4096 * i, i * 32 * 1280>(lbase + P2_W + hw * 1024, wov, rwo, hw * 8 * 1280); });
    }
}

__device__ __forceinline__ void p1_attention(const Params& P, LAS char* L, int cw, int lane, int T0, int p) {
    const int t = lane & 15, gq = lane >> 4;
    const rsrc_t rws = mk_rsrc(P.ws, WS_END);
    const int q = t >> 2, pp = t & 3;
    f32x4 bp[2], bc;
#pragma unroll
    for (int r = 0; r < 4; ++r) {
        const int j = 4 * gq + r; const bool par = ((j ^ t) & 1) == 0;
        const float m = (par && j >= t + 2) ? 0.f : -3.0e38f;
        bp[1][r] = m; bp[0][r] = (T0 + 32 * cw > 0) ? m : -3.0e38f;
        bc[r] = (par && j <= t) ? 0.f : -3.0e38f;
    }
    LAS char* rows[3];
#pragma unroll
    for (int i = 0; i < 3; ++i) { const int tile = 2 * cw + i; rows[i] = L + (tile < 8 ? P1_STG + tile * 16 * P1_RS : P1_STG8); }
    LAS char* ost = L + P1_OST + cw * 2560;
    await<false>(L, C_STG, 4);
    u32x4 qf[2][2], kf[3][2]; bf16x8 vf[2][2][2];
#pragma unroll
    for (int h = 0; h < 2; ++h) {
#pragma unroll
        for (int i = 0; i < 3; ++i) kf[i][h] = *(LAS u32x4*)(rows[i] + t * P1_RS + 96 + h * 48 + gq * 16);
#pragma unroll
        for (int a = 0; a < 2; ++a) qf[a][h] = *(LAS u32x4*)(gq < 3 ? rows[a + 1] + t * P1_RS + h * 48 + gq * 16 : L + CTL + 4 * C_ZERO);
    }
#pragma unroll
    for (int a = 0; a < 2; ++a)
#pragma unroll
        for (int h = 0; h < 2; ++h)
#pragma unroll
            for (int dt = 0; dt < 2; ++dt) {
                const int o = (4 * gq + q) * P1_RS + 192 + h * 40 + pp * 8 + dt * 32;
                vf[a][h][dt] = lds_tr2(rows[a] + o, rows[a + 1] + o);
            }
    f32x4 sp[4], sc[4];
#pragma unroll
    for (int u = 0; u < 4; ++u) {
        const bf16x8 qm = as_frag(qf[u >> 1][u & 1]);
        sp[u] = mfma(as_frag(kf[u >> 1][u & 1]), qm, bp[u >> 1]);
        sc[u] = mfma(as_frag(kf[(u >> 1) + 1][u & 1]), qm, bc);
    }
    const bool odd = t & 1;
    float e[4][4];
#pragma unroll
    for (int u = 0; u < 4; ++u) { e[u][0] = odd ? sp[u][1] : sp[u][0]; e[u][1] = odd ? sp[u][3] : sp[u][2]; e[u][2] = odd ? sc[u][1] : sc[u][0]; e[u][3] = odd ? sc[u][3] : sc[u][2]; }
    SCHED0();
    float mx[4];
#pragma unroll
    for (int u = 0; u < 4; ++u) mx[u] = max3(e[u][0], e[u][1], e[u][2]);
#pragma unroll
    for (int u = 0; u < 4; ++u) mx[u] = max3(mx[u], e[u][3], e[u][3]);
    red4<true>(mx);
    constexpr float SC2 = 0.22360679774997896f * 1.4426950408889634f;
#pragma unroll
    for (int u = 0; u < 4; ++u) mx[u] *= -SC2;
#pragma unroll
    for (int r = 0; r < 4; ++r)
#pragma unroll
        for (int u = 0; u < 4; ++u) e[u][r] = fmaf(e[u][r], SC2, mx[u]);
#pragma unroll
    for (int r = 0; r < 4; ++r)
#pragma unroll
        for (int u = 0; u < 4; ++u) e[u][r] = __builtin_amdgcn_exp2f(e[u][r]);
    const unsigned half = odd ? 0xffff0000u : 0x0000ffffu;
    u32x4 pf[4];
#pragma unroll
    for (int r = 0; r < 4; ++r)
#pragma unroll
        for (int u = 0; u < 4; ++u) pf[u][r] = pk2(e[u][r], e[u][r]) & half;
    f32x4 ov[4][2];
#pragma unroll
    for (int u = 0; u < 4; ++u)
#pragma unroll
        for (int dt = 0; dt < 2; ++dt) { const f32x4 z = {0, 0, 0, 0}; ov[u][dt] = mfma(vf[u >> 1][u & 1][dt], as_frag(pf[u]), z); }
    float sm[4];
#pragma unroll
    for (int u = 0; u < 4; ++u) sm[u] = (e[u][0] + e[u][1]) + (e[u][2] + e[u][3]);
    red4<false>(sm);
    u32x2 ow[2][2][2];
#pragma unroll
    for (int u = 0; u < 4; ++u) {
        const float inv = __builtin_amdgcn_rcpf(sm[u]);
#pragma unroll
        for (int dt = 0; dt < 2; ++dt) { ow[u >> 1][u & 1][dt].x = pk2(ov[u][dt][0] * inv, ov[u][dt][1] * inv); ow[u >> 1][u & 1][dt].y = pk2(ov[u][dt][2] * inv, ov[u][dt][3] * inv); }
    }
#pragma unroll
    for (int a = 0; a < 2; ++a)
#pragma unroll
        for (int h = 0; h < 2; ++h) { const int fo = 20 * h + 4 * gq; *(LAS u32x2*)(ost + a * 1280 + (fo >> 3) * 256 + t * 16 + (fo & 7) * 2) = ow[a][h][0]; }
    if (gq == 0) {
#pragma unroll
        for (int a = 0; a < 2; ++a)
#pragma unroll
            for (int h = 0; h < 2; ++h) { const int fo = 20 * h + 16; *(LAS u32x2*)(ost + a * 1280 + (fo >> 3) * 256 + t * 16 + (fo & 7) * 2) = ow[a][h][1]; }
    }
    asm volatile("" ::: "memory");
    const int gbase = WS_O + (((T0 >> 4) + 2 * cw) * 80 + 5 * p) * 256;
    const u32x4 v0 = *(LAS u32x4*)(ost + lane * 16), v1 = *(LAS u32x4*)(ost + 1280 + lane * 16);
    const int l2 = lane & 15, a2 = (lane >> 4) & 1;
    const u32x4 v2 = *(LAS u32x4*)(ost + a2 * 1280 + 1024 + l2 * 16);
    __builtin_amdgcn_raw_buffer_store_b128(v0, rws, gbase + lane * 16, 0, AUX_SC1);
    __builtin_amdgcn_raw_buffer_store_b128(v1, rws, gbase + 20480 + lane * 16, 0, AUX_SC1);
    if (lane < 32) __builtin_amdgcn_raw_buffer_store_b128(v2, rws, gbase + a2 * 20480 + 1024 + l2 * 16, 0, AUX_SC1);
}

__device__ __forceinline__ void kreduce(LAS char* L, int red, int cword, int wid, int lane, f32x4 (&acc)[3][4], float (&v)[3][8]) {
#pragma unroll
    for (int m = 0; m < 3; ++m)
#pragma unroll
        for (int n = 0; n < 2; ++n) *(LAS f32x4*)(L + red + wid * 6144 + (m * 2 + n) * 1024 + lane * 16) = acc[m][2 + n];
    asm volatile("" ::: "memory");
    sig(L, cword + (wid >> 1), lane);
    await<false>(L, cword + (wid >> 1), 2);
#pragma unroll
    for (int m = 0; m < 3; ++m) {
        f32x4 a = acc[m][0] + *(LAS f32x4*)(L + red + (wid ^ 1) * 6144 + (m * 2 + 0) * 1024 + lane * 16);
        f32x4 b = acc[m][1] + *(LAS f32x4*)(L + red + (wid ^ 1) * 6144 + (m * 2 + 1) * 1024 + lane * 16);
#pragma unroll
        for (int r = 0; r < 4; ++r) { float x = a[r], y = b[r]; swap16f(x, y); v[m][r] = x; v[m][4 + r] = y; }
    }
}

#define RK_STEP(NT, ACUR, BCUR, LOADS, READS)                                                          \
    _Pragma("unroll") for (int m = 0; m < NT; ++m) {                                                   \
        _Pragma("unroll") for (int n = 0; n < 4; ++n) {                                                \
            acc[m][n] = mfma(BCUR[n], as_frag(ACUR[m]), acc[m][n]);                                    \
            SCHED0();                                                                                  \
            { const int i = m * 4 + n; if (i < 8) { READS(i) } if (n == 3) { LOADS(m) } }              \
            SCHED0();                                                                                  \
        }                                                                                              \
    }

__device__ __forceinline__ void p2_gemm(const rsrc_t rws, const unsigned (&ab)[3], LAS char* W, const int (&wb)[4], int lane, f32x4 (&acc)[3][4]) {
    constexpr int NKS = 10, PD = 4, NT = 3;
    u32x4 a[NKS][3];
    const int vo[3] = {(int)(lane * 16 + ab[0]), (int)(lane * 16 + ab[1]), (int)(lane * 16 + ab[2])};
#define P2_KOFF(ks) ((ks) * 1024)
#pragma unroll
    for (int ks = 0; ks < PD; ++ks)
#pragma unroll
        for (int m = 0; m < NT; ++m) a[ks][m] = __builtin_amdgcn_raw_buffer_load_b128(rws, vo[m], P2_KOFF(ks), AUX_SC1);
    s16x4 b[2][8];
#pragma unroll
    for (int i = 0; i < 8; ++i) b[0][i] = lds_tr(W + wb[i >> 1] + (i & 1) * 512);
    SCHED0();
#pragma unroll
    for (int ks = 0; ks < NKS; ++ks) {
        const bf16x8 bc[4] = {cat(b[ks & 1][0], b[ks & 1][1]), cat(b[ks & 1][2], b[ks & 1][3]), cat(b[ks & 1][4], b[ks & 1][5]), cat(b[ks & 1][6], b[ks & 1][7])};
#define P2_READS(i) if (ks + 1 < NKS) b[(ks + 1) & 1][i] = lds_tr(W + wb[(i) >> 1] + ((i) & 1) * 512 + (ks + 1) * 4096);
#define P2_LOADS(m) if (ks + PD < NKS) a[ks + PD][m] = __builtin_amdgcn_raw_buffer_load_b128(rws, vo[m], P2_KOFF(ks + PD), AUX_SC1);
        RK_STEP(NT, a[ks], bc, P2_LOADS, P2_READS)
    }
}

__device__ __forceinline__ void p2_compute(const Params& P, LAS char* L, int cw, int lane, u64 ep, int g, int m_id, int rt, int ct) {
    const int t = lane & 15, gq = lane >> 4, mh = cw >> 1, kh = cw & 1;
    const rsrc_t rws = mk_rsrc(P.ws, WS_END), rx = mk_rsrc(P.x, SEQ * DM * 2);
    int tile0, ntile; rowtile(rt, mh, tile0, ntile);
    const int gt0 = 16 * g + tile0;
    const int col = 64 * ct + 32 * kh + 16 * (gq & 1) + 8 * (gq >> 1);
    u32x4 xr[3];
#pragma unroll
    for (int m = 0; m < 3; ++m) xr[m] = __builtin_amdgcn_raw_buffer_load_b128(rx, (16 * (gt0 + m) + t) * 1280 + col * 2, (m < 2 || ntile == 3) ? 0u : OOB, 0);
    int wb[4];
#pragma unroll
    for (int n = 0; n < 4; ++n) wb[n] = wimg_rd(lane, n ^ (kh << 1));
    f32x4 acc[3][4];
#pragma unroll
    for (int m = 0; m < 3; ++m)
#pragma unroll
        for (int n = 0; n < 4; ++n) acc[m][n] = f32x4{0, 0, 0, 0};
    await<false>(L, C_WO, 4);
    await<false>(L, C_PUB1, 4);
    poll(P.ws, ep, g, 0, (0xffu << (16 * kh + 8 * (tile0 >> 3))) | (0xffu << (16 * kh + 8 * ((tile0 + ntile - 1) >> 3))), lane);
    const unsigned abase = WS_O + (gt0 * 80 + 40 * kh) * 256;
    const unsigned ab[3] = {abase, abase + 20480, ntile == 3 ? abase + 40960 : OOB};
    p2_gemm(rws, ab, L + P2_W + kh * 10 * 4096, wb, lane, acc);
    sig(L, C_P2MAIN, lane);
    float v[3][8];
    kreduce(L, P2_RED, C_RED2, cw, lane, acc, v);
    asm volatile("" : "+v"(v[0][0]), "+v"(v[1][0]), "+v"(v[2][7]));
    u32x4 hp[3]; float st[2][4];
#pragma unroll
    for (int m = 0; m < 3; ++m) {
#pragma unroll
        for (int e = 0; e < 4; ++e) hp[m][e] = pk2(v[m][2 * e] + bflo(xr[m][e]), v[m][2 * e + 1] + bfhi(xr[m][e]));
    }
    const int chunk = 8 * ct + 4 * kh + 2 * (gq & 1) + (gq >> 1);
#pragma unroll
    for (int m = 0; m < 3; ++m) {
        if (m < 2 || ntile == 3) __builtin_amdgcn_raw_buffer_store_b128(hp[m], rws, WS_H + ((gt0 + m) * 80 + chunk) * 256 + t * 16, 0, AUX_SC1);
    }
#pragma unroll
    for (int m = 0; m < 3; ++m) {
        float s = 0.f, q = 0.f;
#pragma unroll
        for (int e = 0; e < 4; ++e) { s = dot2(hp[m][e], 0x3f803f80u, s); q = dot2(hp[m][e], hp[m][e], q); }
        st[0][m] = s; st[1][m] = q;
    }
    st[0][3] = 0.f; st[1][3] = 0.f;
    red4<false>(st[0]); red4<false>(st[1]);
    if (gq < ntile) {
        const float s = gq == 0 ? st[0][0] : (gq == 1 ? st[0][1] : st[0][2]), q = gq == 0 ? st[1][0] : (gq == 1 ? st[1][1] : st[1][2]);
        const u32x2 sv = {asu(s), asu(q)};
        __builtin_amdgcn_raw_buffer_store_b64(sv, rws, WS_ST + (((gt0 + gq) * 20 + 2 * ct + kh) * 16 + t) * 8, 0, AUX_SC1);
    }
    await<false>(L, C_P2MAIN, 4);
#pragma unroll
    for (int m = 0; m < 3; ++m) *(LAS u32x4*)(L + HKEEP + cw * 3072 + m * 1024 + lane * 16) = hp[m];
    WAIT_LGKM0();
    sig(L, C_P2DONE, lane);
    publish(L, P.ws, ep, g, m_id, 1, C_PUB2, 4, lane);
}

__device__ __forceinline__ void w1_voff(int lane, int f, int (&v)[3]) {
#pragma unroll
    for (int b = 0; b < 3; ++b) {
        const int o = b * 1024 + lane * 16, row = o / 192, sl = (o - row * 192) >> 4;
        v[b] = row * 3072 + f * 192 + sl * 16;
    }
}
__device__ __forceinline__ void w1_group(unsigned lbase, const int (&v)[3], rsrc_t rw, int a) {
    const unsigned lb = lbase + P3_W + a * 3072, sb = a * 16 * 3072;
    dma16c<0, 0>(lb, v[0], rw, sb); dma16c<1024, 0>(lb, v[1], rw, sb); dma16c<2048, 0>(lb, v[2], rw, sb);
}

__device__ __forceinline__ void p3_compute(const Params& P, LAS char* L, int cw, int lane, u64 ep, int g, int m_id, int tt, int f) {
    const int hh = lane >> 5;
    const rsrc_t rws = mk_rsrc(P.ws, WS_END);
    const int gt0 = 16 * g + 8 * tt + 2 * cw;
    const int wb = w1_rd32(lane);
    LAS char* W = L + P3_W + wb;
    f32x16 acc[3];
#pragma unroll
    for (int J = 0; J < 3; ++J)
#pragma unroll
        for (int r = 0; r < 16; ++r) acc[J][r] = 0.f;
    constexpr int NKS = 40, PD = 10;
    u32x4 a[NKS];
    const int vo = WS_H + gt0 * 20480 + ((lane >> 4) & 1) * 20480 + hh * 256 + (lane & 15) * 16;
#define P3_KOFF(ks) ((ks) * 512)
    await<false>(L, C_W1, 3);
    s16x4 b[2][6];
#pragma unroll
    for (int i = 0; i < 6; ++i) b[0][i] = lds_tr(W + (i >> 1) * 64 + (i & 1) * 768);
    {
        const int a0 = 8 * tt + 2 * cw, r0 = a0 < 6 ? 0 : (a0 < 11 ? 1 : 2), r1 = a0 + 1 < 6 ? 0 : (a0 + 1 < 11 ? 1 : 2);
        poll(P.ws, ep, g, 1, (0x001f001fu << (5 * r0)) | (0x001f001fu << (5 * r1)), lane);
    }
#pragma unroll
    for (int ks = 0; ks < PD; ++ks) a[ks] = __builtin_amdgcn_raw_buffer_load_b128(rws, vo, P3_KOFF(ks), AUX_SC1);
    SCHED0();
#pragma unroll
    for (int ks = 0; ks < NKS; ++ks) {
#pragma unroll
        for (int J = 0; J < 3; ++J) {
            acc[J] = mfma32(cat(b[ks & 1][2 * J], b[ks & 1][2 * J + 1]), as_frag(a[ks]), acc[J]);
            SCHED0();
            if (ks + 1 < NKS) {
                b[(ks + 1) & 1][2 * J] = lds_tr(W + J * 64 + (ks + 1) * 3072);
                b[(ks + 1) & 1][2 * J + 1] = lds_tr(W + J * 64 + 768 + (ks + 1) * 3072);
            }
            if (J == 0 && ks + PD < NKS) a[ks + PD] = __builtin_amdgcn_raw_buffer_load_b128(rws, vo, P3_KOFF(ks + PD), AUX_SC1);
            SCHED0();
        }
    }
    sig(L, C_P3DONE, lane);
    await<false>(L, C_CS, 3);
    await<false>(L, C_ST3, 1);
    const f32x2 rw = *(LAS f32x2*)(L + P3_ROWTAB + (32 * cw + (lane & 31)) * 8);
    asm volatile("s_nop 7\n\ts_nop 7\n\ts_nop 3" ::: "memory");
    SCHED0();
#pragma unroll
    for (int J = 0; J < 3; ++J) {
        f32x4 cs[4], bi[4];
#pragma unroll
        for (int i = 0; i < 4; ++i) { cs[i] = *(LAS f32x4*)(L + CTL_CS + (32 * J + 8 * i + 4 * hh) * 4); bi[i] = *(LAS f32x4*)(L + P3_BIAS + (32 * J + 8 * i + 4 * hh) * 4); }
        f32x2 y[4][2];
#pragma unroll
        for (int i = 0; i < 4; ++i)
#pragma unroll
            for (int u = 0; u < 2; ++u) {
                const f32x2 c2 = {cs[i][2 * u], cs[i][2 * u + 1]}, b2 = {bi[i][2 * u], bi[i][2 * u + 1]};
                asm("v_pk_fma_f32 %0, %1, %2, %3 op_sel:[0,1,0] op_sel_hi:[1,1,1]" : "=v"(y[i][u]) : "v"(c2), "v"(rw), "v"(b2));
            }
        SCHED0();
#pragma unroll
        for (int i = 0; i < 4; ++i)
#pragma unroll
            for (int u = 0; u < 2; ++u) {
                const f32x2 a2 = {acc[J][4 * i + 2 * u], acc[J][4 * i + 2 * u + 1]};
                asm("v_pk_fma_f32 %0, %1, %2, %3 op_sel_hi:[1,0,1]" : "=v"(y[i][u]) : "v"(a2), "v"(rw), "v"(y[i][u]));
            }
        SCHED0();
#pragma unroll
        for (int i = 0; i < 4; ++i)
#pragma unroll
            for (int u = 0; u < 2; ++u) { y[i][u].x = max2(y[i][u].x, 0.f); y[i][u].y = max2(y[i][u].y, 0.f); }
        SCHED0();
#pragma unroll
        for (int i = 0; i < 4; ++i)
#pragma unroll
            for (int u = 0; u < 2; ++u) asm("v_pk_mul_f32 %0, %1, %1" : "=v"(y[i][u]) : "v"(y[i][u]));
        SCHED0();
        unsigned d[4][2];
#pragma unroll
        for (int i = 0; i < 4; ++i) { d[i][0] = pk2(y[i][0].x, y[i][0].y); d[i][1] = pk2(y[i][1].x, y[i][1].y); }
#pragma unroll
        for (int ip = 0; ip < 2; ++ip) {
            swap32u(d[2 * ip][0], d[2 * ip + 1][0]); swap32u(d[2 * ip][1], d[2 * ip + 1][1]);
            const u32x4 o = {d[2 * ip][0], d[2 * ip][1], d[2 * ip + 1][0], d[2 * ip + 1][1]};
            const int chunk = 12 * f + 4 * J + 2 * ip + hh;
            __builtin_amdgcn_raw_buffer_store_b128(o, rws, WS_H1 + ((gt0 + ((lane >> 4) & 1)) * 192 + chunk) * 256 + (lane & 15) * 16, 0, AUX_SC1);
        }
        SCHED0();
    }
    publish(L, P.ws, ep, g, m_id, 2, C_PUB3, 4, lane);
}

__device__ __forceinline__ void p3_rowtable(const Params& P, LAS char* L, int lane, int g, int tt) {
    const rsrc_t rws = mk_rsrc(P.ws, WS_END);
    const int vo = WS_ST + (16 * g + 8 * tt + (lane >> 3)) * 2560 + (lane & 7) * 16;
    u32x4 v[20];
#pragma unroll
    for (int i = 0; i < 20; ++i) v[i] = __builtin_amdgcn_raw_buffer_load_b128(rws, vo, i * 128, AUX_SC1);
    float s0 = 0.f, q0 = 0.f, s1 = 0.f, q1 = 0.f;
#pragma unroll
    for (int i = 0; i < 20; ++i) { s0 += asf(v[i][0]); q0 += asf(v[i][1]); s1 += asf(v[i][2]); q1 += asf(v[i][3]); }
    const float m0 = s0 * (1.f / DM), m1 = s1 * (1.f / DM);
    const float r0 = __builtin_amdgcn_rsqf(fmaxf(q0 * (1.f / DM) - m0 * m0, 0.f) + LN_EPS), r1 = __builtin_amdgcn_rsqf(fmaxf(q1 * (1.f / DM) - m1 * m1, 0.f) + LN_EPS);
    *(LAS f32x4*)(L + P3_ROWTAB + lane * 16) = f32x4{r0, -m0 * r0, r1, -m1 * r1};
    WAIT_LGKM0();
    sig(L, C_ST3, lane);
}

__device__ __forceinline__ void p4_gemm(LAS char* L, const rsrc_t rws, const unsigned (&ab)[3], LAS char* WA, LAS char* WB, const int (&wb)[4], int lane,
                                        const s16x4 (&pre)[4][8], f32x4 (&acc)[3][4], unsigned& seen_main) {
    constexpr int NKS = 24, PD = 4, NT = 3;
    u32x4 a[NKS][3];
    const int vo[3] = {(int)(lane * 16 + ab[0]), (int)(lane * 16 + ab[1]), (int)(lane * 16 + ab[2])};
#define P4_KOFF(ks) ((ks) * 1024)
#pragma unroll
    for (int ks = 0; ks < PD; ++ks)
#pragma unroll
        for (int m = 0; m < NT; ++m) a[ks][m] = __builtin_amdgcn_raw_buffer_load_b128(rws, vo[m], P4_KOFF(ks), AUX_SC1);
    s16x4 b[2][8];
    SCHED0();
#pragma unroll
    for (int ks = 0; ks < NKS; ++ks) {
        if (ks == 21) await<false>(L, C_W2LAST, 3);
        bf16x8 bc[4];
#pragma unroll
        for (int n = 0; n < 4; ++n) bc[n] = ks < 4 ? cat(pre[ks < 4 ? ks : 0][2 * n], pre[ks < 4 ? ks : 0][2 * n + 1]) : cat(b[ks & 1][2 * n], b[ks & 1][2 * n + 1]);
#define P4_KL(k1) ((k1) < 18 ? (k1) : (k1) - 18)
#define P4_READS(i) if (ks + 1 >= 4 && ks + 1 < NKS) b[(ks + 1) & 1][i] = lds_tr((P4_KL(ks + 1) < 6 ? WA : WB) + wb[(i) >> 1] + ((i) & 1) * 512 + P4_KL(ks + 1) * 4096);
#define P4_LOADS(m) if (ks + PD < NKS) a[ks + PD][m] = __builtin_amdgcn_raw_buffer_load_b128(rws, vo[m], P4_KOFF(ks + PD), AUX_SC1);
        RK_STEP(NT, a[ks], bc, P4_LOADS, P4_READS)
        if (ks == 11) { asm volatile("" ::: "memory"); sig(L, C_P4MAIN, lane); }
        if (ks == 20) seen_main = peek(L, C_P4MAIN);
        if (ks == 5) { asm volatile("" ::: "memory"); sig(L, C_BLK2, lane); }
    }
}

__device__ __forceinline__ void p4_compute(const Params& P, LAS char* L, int cw, int lane, u64 ep, int g, int rt, int ct) {
    const int t = lane & 15, gq = lane >> 4, mh = cw >> 1, kh = cw & 1;
    const rsrc_t rws = mk_rsrc(P.ws, WS_END), rb = mk_rsrc(P.b2, DM * 2), ro = mk_rsrc(P.out, SEQ * DM * 2);
    int tile0, ntile; rowtile(rt, mh, tile0, ntile);
    const int gt0 = 16 * g + tile0;
    const int col = 64 * ct + 32 * kh + 16 * (gq & 1) + 8 * (gq >> 1);
    int wb[4];
#pragma unroll
    for (int n = 0; n < 4; ++n) wb[n] = wimg_rd(lane, n ^ (kh << 1));
    LAS char* W = L + kh * 15 * 8192;
    LAS char* WB = L + kh * 6 * 8192;
    await<false>(L, C_W2PRE, 1);
    s16x4 pre[4][8];
#pragma unroll
    for (int ks = 0; ks < 4; ++ks)
#pragma unroll
        for (int i = 0; i < 8; ++i) pre[ks][i] = lds_tr(W + wb[i >> 1] + (i & 1) * 512 + ks * 4096);
    WAIT_LGKM0();
    sig(L, C_PREDONE, lane);
    f32x4 acc[3][4];
#pragma unroll
    for (int m = 0; m < 3; ++m)
#pragma unroll
        for (int n = 0; n < 4; ++n) acc[m][n] = f32x4{0, 0, 0, 0};
    await<false>(L, C_W2, 3);
    poll(P.ws, ep, g, 2, (0xffu << (16 * kh + 8 * (tile0 >> 3))) | (0xffu << (16 * kh + 8 * ((tile0 + ntile - 1) >> 3))), lane);
    const unsigned abase = WS_H1 + (gt0 * 192 + 96 * kh) * 256;
    const unsigned ab[3] = {abase, abase + 49152, ntile == 3 ? abase + 98304 : OOB};
    unsigned seen_main = 0;
    p4_gemm(L, rws, ab, W, WB, wb, lane, pre, acc, seen_main);
    const u32x4 bias = __builtin_amdgcn_raw_buffer_load_b128(rb, col * 2, 0, 0);
    await_seen(L, C_P4MAIN, 4, seen_main);
    float v[3][8];
    kreduce(L, P4_RED, C_RED4, cw, lane, acc, v);
#pragma unroll
    for (int m = 0; m < 3; ++m)
        if (m < 2 || ntile == 3) {
            const u32x4 hk = *(LAS u32x4*)(L + HKEEP + cw * 3072 + m * 1024 + lane * 16);
            u32x4 o;
#pragma unroll
            for (int e = 0; e < 4; ++e)
                o[e] = pk2(v[m][2 * e] + bflo(bias[e]) + bflo(hk[e]), v[m][2 * e + 1] + bfhi(bias[e]) + bfhi(hk[e]));
            __builtin_amdgcn_raw_buffer_store_b128(o, ro, (16 * (gt0 + m) + t) * 1280 + col * 2, 0, AUX_SC1);
        }
}

__device__ __forceinline__ void w2_block(unsigned lbase, int va, int vb, rsrc_t rw, int lw, int h, int pos, int blk) {
    const int slot = h == 0 ? pos : (pos < 3 ? 15 + pos : 6 + pos);
    const unsigned lb = lbase + slot * 8192 + lw * 1024, sb = ((12 * h + blk) * 64 + 8 * lw) * 1280;
    dma16c<0, 0>(lb, va, rw, sb);
    dma16c<3072, 24 * 1280>(lb, vb, rw, sb);
    if (lw < 2) dma16c<6144, 48 * 1280>(lb, va, rw, sb);
}

__device__ __forceinline__ void w1_late_load(const Params& P, int lw, int lane, int f, u32x4 (&st)[9][3]) {
    const rsrc_t rw1 = mk_rsrc(P.w1, DM * FF * 2);
    int v1[3]; w1_voff(lane, f, v1);
#pragma unroll
    for (int i = 0; i < 9; ++i) {
        const int a = P3_EARLY + lw + 3 * i;
#pragma unroll
        for (int b = 0; b < 3; ++b) st[i][b] = __builtin_amdgcn_raw_buffer_load_b128(rw1, v1[b], a < 40 ? (unsigned)(a * 16 * 3072) : OOB, 0);
    }
}
__device__ __forceinline__ void loader_tail(const Params& P, LAS char* L, int lw, int lane, bool has24, int f, int ct, int w2j, const u32x4 (&st)[9][3]) {
    const unsigned lbase = (unsigned)(uintptr_t)L;
    const rsrc_t rw1 = mk_rsrc(P.w1, DM * FF * 2), rw2 = mk_rsrc(P.w2, FF * DM * 2);
    int v1[3]; w1_voff(lane, f, v1);
    await<true>(L, C_PUB1, 4);
#pragma unroll 1
    for (int a = lw; a < P3_EARLY; a += 3) w1_group(lbase, v1, rw1, a);
    await<true>(L, C_P2DONE, 4);
#pragma unroll
    for (int i = 0; i < 9; ++i) {
        const int a = P3_EARLY + lw + 3 * i;
        if (a < 40) {
#pragma unroll
            for (int b = 0; b < 3; ++b) *(LAS u32x4*)(L + P3_W + (a * 3 + b) * 1024 + lane * 16) = st[i][b];
        }
    }
    WAIT_VM(0); WAIT_LGKM0();
    sig(L, C_W1, lane);
    const int va = wimg_voff(lw & 1, lane, ct), vb = wimg_voff((lw & 1) ^ 1, lane, ct);
    if (has24) {
#pragma unroll 1
        for (int b = 0; b < 3; ++b) w2_block(lbase, va, vb, rw2, lw, 1, b, b);
    }
    const float b1v = bflo(__builtin_amdgcn_raw_buffer_load_b16(mk_rsrc(P.b1, FF * 2), (96 * f + 32 * lw + (lane & 31)) * 2, 0, 0));
    await<true>(L, C_W1, 3);
    {
        LAS char* W = L + P3_W + w1_rd32(lane) + lw * 64;
        float c[8];
#pragma unroll
        for (int i = 0; i < 8; ++i) c[i] = 0.f;
#pragma unroll 1
        for (int k0 = 0; k0 < 40; k0 += 8) {
            s16x4 v[8][2];
#pragma unroll
            for (int i = 0; i < 8; ++i) { v[i][0] = lds_tr(W + (k0 + i) * 3072); v[i][1] = lds_tr(W + (k0 + i) * 3072 + 768); }
#pragma unroll
            for (int e = 0; e < 4; ++e)
#pragma unroll
                for (int i = 0; i < 8; ++i) { const u32x2 u = __builtin_bit_cast(u32x2, v[i][e >> 1]); c[i] = dot2((e & 1) ? u.y : u.x, 0x3f803f80u, c[i]); }
        }
        const float cs = red_sum32(((c[0] + c[1]) + (c[2] + c[3])) + ((c[4] + c[5]) + (c[6] + c[7])));
        if (lane < 32) { *(LAS float*)(L + CTL_CS + (32 * lw + lane) * 4) = cs; *(LAS float*)(L + P3_BIAS + (32 * lw + lane) * 4) = b1v; }
        WAIT_VM(0); WAIT_LGKM0();
        sig(L, C_CS, lane);
    }
    if (!has24) return;
    {
        const int rows = 1536 / 6, r0 = w2j * rows;
#pragma unroll 1
        for (int i = lw; i < rows / 64; i += 3)
            touch1((unsigned)(uintptr_t)L + TOUCH_LDS2, (const char*)P.w2 + (size_t)(r0 + 64 * i + lane) * 1280 + ct * 128);
    }
    await<true>(L, C_CS, 3);
    await<true>(L, C_P3DONE, 4);
    if (lw == 0) {
        const int v0 = wimg_voff(0, lane, ct), v1 = wimg_voff(1, lane, ct);
        static_for<16>([&](auto I) { constexpr int i = I.value; dma16c<1024 * i, (i >> 3) * 64 * 1280 + (i & 7) * 8 * 1280>(lbase, (i & 1) ? v1 : v0, rw2, 0); });
        WAIT_VM(0);
        sig(L, C_W2PRE, lane);
    }
#pragma unroll 1
    for (int b = 2; b < 9; ++b) w2_block(lbase, va, vb, rw2, lw, 0, b, b);
#pragma unroll 1
    for (int b = 3; b < 9; ++b) w2_block(lbase, va, vb, rw2, lw, 1, b, b);
    await<true>(L, C_PREDONE, 4);
#pragma unroll 1
    for (int i = 0; i < 4; ++i) w2_block(lbase, va, vb, rw2, lw, i >> 1, i & 1, 9 + (i & 1));
    WAIT_VM(0); WAIT_LGKM0();
    sig(L, C_W2, lane);
    await<true>(L, C_BLK2, 4);
#pragma unroll 1
    for (int h = 0; h < 2; ++h) w2_block(lbase, va, vb, rw2, lw, h, 2, 11);
    WAIT_VM(0);
    sig(L, C_W2LAST, lane);
}

__global__ void __launch_bounds__(512, 2) k_block(Params P) {
    extern __shared__ __attribute__((aligned(16))) char smem[];
    LAS char* L = (LAS char*)smem;
    const int tid = threadIdx.x, lane = tid & 63, wid = rfl(tid >> 6);
    const Ids id = decode_block();
    const int g = id.g, m_id = id.half * 16 + id.idx;
    const int tt = id.idx >> 3, pf = id.half * 8 + (id.idx & 7);
    const bool has24 = id.idx < 15;
    const int rt = id.idx / 5, ct = has24 ? id.half * 5 + id.idx % 5 : 0;
    const int T0 = 256 * g + 128 * tt;
    if (wid == 0 && lane < 32) *ctl(L, lane) = 0u;
    if (wid == 2 || wid == 3) {
        for (int i = tid - 128; i < 16 * P1_RS / 16; i += 128) *(LAS u32x4*)(L + P1_STG8 + i * 16) = u32x4{0u, 0u, 0u, 0u};
    }
    if (wid == 1 && lane < 32) *(LAS u32x4*)(L + (lane >> 3) * P1_SLOT + (lane & 7) * 16) = u32x4{0x3f803f80u, 0x3f803f80u, 0x3f803f80u, 0x3f803f80u};
    if (wid < 4) {
        __builtin_amdgcn_s_setprio(3);
        p1_compute(P, L, wid, lane, g, m_id, T0, pf);
        const u64 ep = *(LAS u64*)(L + CTL_E);
        p1_attention(P, L, wid, lane, T0, pf);
        publish(L, P.ws, ep, g, m_id, 0, C_PUB1, 4, lane);
        if (has24) p2_compute(P, L, wid, lane, ep, g, m_id, rt, ct);
        else sig(L, C_P2DONE, lane);
        p3_compute(P, L, wid, lane, ep, g, m_id, tt, pf);
        if (has24) p4_compute(P, L, wid, lane, ep, g, rt, ct);
    } else {
        const int hw = wid - 4;
        p1_helper(P, L, hw, lane, g, m_id, tt, T0, pf, has24, ct);
        u32x4 st[9][3];
        asm volatile("" ::: "memory");
        if (hw >= 1) w1_late_load(P, hw - 1, lane, pf, st);
        if (hw >= 1) WAIT_VM(27); else WAIT_VM(0);
        sig(L, C_WO, lane);
        if (hw == 0) {
            const u64 ep = *(LAS u64*)(L + CTL_E);
            await<true>(L, has24 ? C_PUB2 : C_P2DONE, 4);
            poll(P.ws, ep, g, 1, tt == 0 ? 0x03ff03ffu : 0x7fe07fe0u, lane);
            p3_rowtable(P, L, lane, g, tt);
            if (m_id == 15) { await<true>(L, C_PUB3, 4); poll(P.ws, ep, g, 2, 0xffffffffu, lane); }
            if (m_id == 15 && lane == 0) __hip_atomic_store((GAS u64*)((char*)P.ws + WS_CTRL + g * 32768), *(LAS u64*)(L + CTL_E) + 8, __ATOMIC_RELAXED, __HIP_MEMORY_SCOPE_AGENT);
        } else {
            loader_tail(P, L, hw - 1, lane, has24, pf, ct, 3 * (g & 1) + rt, st);
        }
    }
}

}

extern "C" void kernel_launch(void* const* d_in, const int* in_sizes, int n_in, void* d_out, int out_size, void* d_ws, size_t ws_size, hipStream_t stream) {
    Params P;
    P.x = d_in[0]; P.wqkv = d_in[1]; P.wo = d_in[2]; P.w1 = d_in[4]; P.b1 = d_in[5]; P.w2 = d_in[6]; P.b2 = d_in[7];
    P.out = d_out; P.ws = d_ws;
    (void)hipFuncSetAttribute((const void*)k_block, hipFuncAttributeMaxDynamicSharedMemorySize, LDS_BYTES);
    k_block<<<256, 512, LDS_BYTES, stream>>>(P);
}
```
